```python
import torch

def _softmax(x, axis=-1):
    return torch.softmax(x, dim=axis)
def _gelu(x):
    return torch.nn.functional.gelu(x, approximate="tanh")
def _silu(x):
    return torch.nn.functional.silu(x)
def _sigmoid(x):
    return torch.sigmoid(x)
def _layernorm(x, axis=-1, eps=1e-5):
    m = x.mean(dim=axis, keepdim=True)
    v = x.var(dim=axis, keepdim=True, unbiased=False)
    return (x - m) / torch.sqrt(v + eps)
def _scatter_add(tgt, idx, src, axis=0):
    return tgt.clone().index_add_(axis, idx, src)
def _segment_sum(src, seg, n):
    out = torch.zeros((n,) + src.shape[1:], dtype=src.dtype)
    return out.index_add_(0, seg, src)
def _pad(x, pw):
    flat = tuple(v for pair in reversed(pw) for v in pair)
    return torch.nn.functional.pad(x, flat)

kernel_name = 'parallel_sum__ssm_conv_s5__mixer_channel_mlp__prompt'

def setup_inputs():

    _w = (lambda a: (lambda t: t.to(torch.bfloat16) if t.is_floating_point() else t)(torch.as_tensor(a)))
    def _A_setup():
        B, S, D = 1, 2048, 512
        P = D
        Lambda_re = -0.5 * (lambda _x: torch.exp(torch.as_tensor(_x)))((lambda *s: torch.randn(*s, dtype=torch.bfloat16) * 0.2)(P))
        Lambda_im = (lambda *s: torch.randn(*s, dtype=torch.bfloat16) * 0.8)(P) * 1.0
        B_re = (lambda *s: torch.randn(*s, dtype=torch.bfloat16) * 0.2)(P, D) * (D ** -0.5)
        B_im = (lambda *s: torch.randn(*s, dtype=torch.bfloat16) * 0.2)(P, D) * (D ** -0.5)
        C_re = (lambda *s: torch.randn(*s, dtype=torch.bfloat16) * 0.2)(D, P) * (P ** -0.5)
        C_im = (lambda *s: torch.randn(*s, dtype=torch.bfloat16) * 0.2)(D, P) * (P ** -0.5)
        log_dt = (lambda *s: torch.randn(*s, dtype=torch.bfloat16) * 0.2)(P) * 0.3 - 2.0
        Dskip = (lambda *s: torch.randn(*s, dtype=torch.bfloat16) * 0.05)(D)
        return {
            "x": _w((lambda *s: torch.randn(*s, dtype=torch.bfloat16) * 0.05)(B, S, D)),
            "Lambda_re": _w(Lambda_re),
            "Lambda_im": _w(Lambda_im),
            "B_re": _w(B_re),
            "B_im": _w(B_im),
            "C_re": _w(C_re),
            "C_im": _w(C_im),
            "log_dt": _w(log_dt),
            "Dskip": _w(Dskip),
        }
    _A_in = _A_setup()

    def _B_setup():
        B, S, D, F = 1, 2048, 512, 1376
        return {
            "x": _w((lambda *s: torch.randn(*s, dtype=torch.bfloat16) * 0.05)(B, S, D)),
            "w1": _w((lambda *s: torch.randn(*s, dtype=torch.bfloat16) * 1.6)(D, F) * (D ** -0.5)),
            "b1": _w((lambda *s: torch.randn(*s, dtype=torch.bfloat16) * 0.8)(F)),
            "w2": _w((lambda *s: torch.randn(*s, dtype=torch.bfloat16) * 0.05)(F, D) * (F ** -0.5)),
            "b2": _w((lambda *s: torch.randn(*s, dtype=torch.bfloat16) * 0.05)(D)),
        }
    _B_in = _B_setup()

    return {
        "A_x": _A_in["x"],
        "A_Lambda_re": _A_in["Lambda_re"],
        "A_Lambda_im": _A_in["Lambda_im"],
        "A_B_re": _A_in["B_re"],
        "A_B_im": _A_in["B_im"],
        "A_C_re": _A_in["C_re"],
        "A_C_im": _A_in["C_im"],
        "A_log_dt": _A_in["log_dt"],
        "A_Dskip": _A_in["Dskip"],
        "B_x": _B_in["x"],
        "B_w1": _B_in["w1"],
        "B_b1": _B_in["b1"],
        "B_w2": _B_in["w2"],
        "B_b2": _B_in["b2"],
    }

def reference(A_x, A_Lambda_re, A_Lambda_im, A_B_re, A_B_im, A_C_re, A_C_im, A_log_dt, A_Dskip, B_x, B_w1, B_b1, B_w2, B_b2):
    def _A_ref(x, Lambda_re, Lambda_im, B_re, B_im, C_re, C_im, log_dt, Dskip):
        Bsz, L, H = x.shape
        Lam = Lambda_re + 1j * Lambda_im
        Bmat = B_re + 1j * B_im
        Cmat = C_re + 1j * C_im
        dt = torch.exp(log_dt)
        Lam_bar = torch.exp(dt * Lam)
        B_bar = ((Lam_bar - 1.0) / Lam)[:, None] * Bmat
        Bu = torch.einsum("bld,pd->blp", x + 0j, B_bar)
        h = torch.zeros_like(Bu[:, 0, :])
        hs = []
        for t in range(L):
            h = Lam_bar[None, :] * h + Bu[:, t, :]
            hs.append(h)
        hseq = torch.stack(hs, 1)
        y = 2.0 * torch.einsum("blp,dp->bld", hseq, Cmat).real + x * Dskip[None, None, :]
        return y

    def _B_ref(x, w1, b1, w2, b2):
        y = _layernorm(x, axis=-1)
        h = _gelu(torch.einsum("bsd,df->bsf", y, w1) + b1)
        return torch.einsum("bsf,fd->bsd", h, w2) + b2

    x = A_x
    xn = _layernorm(x, axis=-1)
    return x + _A_ref(xn, A_Lambda_re, A_Lambda_im, A_B_re, A_B_im, A_C_re, A_C_im, A_log_dt, A_Dskip) + _B_ref(xn, B_w1, B_b1, B_w2, B_b2)
```

```cpp
#include <hip/hip_runtime.h>
#include <hip/hip_bf16.h>

typedef unsigned short u16;
typedef unsigned u32;
typedef unsigned long long u64;
typedef __bf16 bf16x8 __attribute__((ext_vector_type(8)));
typedef __bf16 bf16x2 __attribute__((ext_vector_type(2)));
typedef short s16x4 __attribute__((ext_vector_type(4)));
typedef float f32x16 __attribute__((ext_vector_type(16)));
typedef float f32x4 __attribute__((ext_vector_type(4)));
typedef float f32x2 __attribute__((ext_vector_type(2)));
typedef u32 u32x4 __attribute__((ext_vector_type(4)));
typedef u32 u32x2 __attribute__((ext_vector_type(2)));
#define LAS __attribute__((address_space(3)))
#define GAS __attribute__((address_space(1)))
#define RLX_AGENT __ATOMIC_RELAXED, __HIP_MEMORY_SCOPE_AGENT
#define RLX_WG __ATOMIC_RELAXED, __HIP_MEMORY_SCOPE_WORKGROUP
#define DEVI __device__ __forceinline__

constexpr int FF = 1376;
constexpr int NKS = 150;

constexpr u32 WS_EPOCH = 0;
constexpr u32 WS_GRAN = 4096;
constexpr u32 WS_FLAGM = WS_GRAN + 131072;
constexpr u32 WS_FLAGS = WS_FLAGM + 32768;
constexpr u32 WS_STAT = WS_FLAGS + 32768;
constexpr u32 WS_H = 262144;
constexpr u32 H_BYTES = 64u * NKS * 1024u;

constexpr u32 A_X = 0, A_B = 65536, A_W = 98304;
constexpr u32 SL_X = 16384, SL_B = 8192, SL_W = 12288;
constexpr u32 REG_S = 16384, REG_M0 = 65536, REG_M = 22528;
constexpr u32 MISC = 155648;
constexpr u32 L_JUNK = MISC;
constexpr u32 L_B1R = MISC + 256;
constexpr u32 L_DB = MISC + 512;
constexpr u32 L_R1 = MISC + 768;
constexpr u32 L_RM = L_R1 + 512;
constexpr u32 L_STG = L_RM + 512;
constexpr u32 L_PS = L_STG + 2048;
constexpr u32 L_PM = L_PS + 1024;
constexpr u32 L_WAG = L_PM + 1536;
constexpr u32 L_CNT = L_WAG + 1024;
constexpr u32 L_CIN = L_CNT + 64;
constexpr u32 LDS_BYTES = 163840;
static_assert(L_CIN + 256 <= LDS_BYTES, "LDS map");

constexpr u32 SPIN_LIMIT = 1u << 20;
constexpr int MWIN = 8, SWIN = 10;
constexpr int WARM_U = 24;

DEVI float bf2f(u32 v) { return __uint_as_float(v << 16); }
DEVI u32 pack2(float a, float b) { bf16x2 p = {(__bf16)a, (__bf16)b}; return __builtin_bit_cast(u32, p); }
DEVI float dsum(u32 a, float c) {
    return __builtin_amdgcn_fdot2_f32_bf16(__builtin_bit_cast(bf16x2, a), __builtin_bit_cast(bf16x2, 0x3F803F80u), c, false);
}
DEVI f32x16 mfma(u32x4 a, u32x4 b, f32x16 c) {
    return __builtin_amdgcn_mfma_f32_32x32x16_bf16(__builtin_bit_cast(bf16x8, a), __builtin_bit_cast(bf16x8, b), c, 0, 0, 0);
}
DEVI void dma16(const void* sbase, u32 voff, u32 lds) {
    asm volatile("s_mov_b32 m0, %2\n\ts_nop 0\n\tglobal_load_lds_dwordx4 %0, %1" ::"v"(voff), "s"(sbase), "s"(lds) : "memory");
}
DEVI void dma16c(const void* sbase, u32 voff, u32 lds) {
    asm volatile("s_mov_b32 m0, %2\n\ts_nop 0\n\tglobal_load_lds_dwordx4 %0, %1 sc1" ::"v"(voff), "s"(sbase), "s"(lds) : "memory");
}
DEVI void dma4v(const void* vaddr, u32 lds) {
    asm volatile("s_mov_b32 m0, %1\n\ts_nop 0\n\tglobal_load_lds_dword %0, off sc1" ::"v"(vaddr), "s"(lds) : "memory");
}
template <int N> DEVI void wait_vm() { asm volatile("s_waitcnt vmcnt(%0)" ::"i"(N) : "memory"); }
template <int N> DEVI void wait_vm_barrier() { asm volatile("s_waitcnt vmcnt(%0) lgkmcnt(0)\n\ts_barrier" ::"i"(N) : "memory"); }
DEVI void wait_lgkm() { asm volatile("s_waitcnt lgkmcnt(0)" ::: "memory"); }
DEVI void barrier_lds() { asm volatile("s_waitcnt lgkmcnt(0)\n\ts_barrier" ::: "memory"); }
DEVI u32x4 lds128(LAS char* ls, u32 a) { return *(LAS u32x4*)(ls + a); }
DEVI f32x4 ldsf4(LAS char* ls, u32 a) { return *(LAS f32x4*)(ls + a); }
DEVI float ldsf(LAS char* ls, u32 a) { return *(LAS float*)(ls + a); }
DEVI u32x4 ldstr(LAS char* ls, u32 a0, u32 a1) {
    u32x2 t0 = __builtin_bit_cast(u32x2, __builtin_amdgcn_ds_read_tr16_b64_v4i16((LAS s16x4*)(ls + a0)));
    u32x2 t1 = __builtin_bit_cast(u32x2, __builtin_amdgcn_ds_read_tr16_b64_v4i16((LAS s16x4*)(ls + a1)));
    return (u32x4){t0.x, t0.y, t1.x, t1.y};
}
DEVI u32x4 ldstrp(LAS char* p, int o0, int o1) {
    u32x2 t0 = __builtin_bit_cast(u32x2, __builtin_amdgcn_ds_read_tr16_b64_v4i16((LAS s16x4*)(p + o0)));
    u32x2 t1 = __builtin_bit_cast(u32x2, __builtin_amdgcn_ds_read_tr16_b64_v4i16((LAS s16x4*)(p + o1)));
    return (u32x4){t0.x, t0.y, t1.x, t1.y};
}
DEVI void halves(float v, float& lo, float& hi) {
    auto r = __builtin_amdgcn_permlane32_swap(__float_as_uint(v), __float_as_uint(v), false, false);
    lo = __uint_as_float(r[0]);
    hi = __uint_as_float(r[1]);
}
DEVI float both_halves_sum(float v) { float lo, hi; halves(v, lo, hi); return lo + hi; }
DEVI float lo16(u32 v) { return __uint_as_float(v << 16); }
DEVI float hi16(u32 v) { return __uint_as_float(v & 0xffff0000u); }
DEVI float wave_sum63(float v) {
    v += __uint_as_float(__builtin_amdgcn_update_dpp(0u, __float_as_uint(v), 0x111, 0xf, 0xf, true));
    v += __uint_as_float(__builtin_amdgcn_update_dpp(0u, __float_as_uint(v), 0x112, 0xf, 0xf, true));
    v += __uint_as_float(__builtin_amdgcn_update_dpp(0u, __float_as_uint(v), 0x114, 0xf, 0xf, true));
    v += __uint_as_float(__builtin_amdgcn_update_dpp(0u, __float_as_uint(v), 0x118, 0xf, 0xf, true));
    v += __uint_as_float(__builtin_amdgcn_update_dpp(0u, __float_as_uint(v), 0x142, 0xa, 0xf, true));
    v += __uint_as_float(__builtin_amdgcn_update_dpp(0u, __float_as_uint(v), 0x143, 0xc, 0xf, true));
    return v;
}
#define SB() __builtin_amdgcn_sched_barrier(0)
#define PIN(x) asm volatile("" : "+v"(x))
#define PIN8(a, o) asm volatile("" : "+v"(a[o]), "+v"(a[o + 1]), "+v"(a[o + 2]), "+v"(a[o + 3]), "+v"(a[o + 4]), "+v"(a[o + 5]), "+v"(a[o + 6]), "+v"(a[o + 7]))
#define PIN16(a) do { PIN8(a, 0); PIN8(a, 8); } while (0)
DEVI int opaque(int v) { asm volatile("" : "+v"(v)); return v; }
struct cf { float r, i; };
DEVI cf cmul(cf a, cf b) { return {a.r * b.r - a.i * b.i, a.r * b.i + a.i * b.r}; }
DEVI cf cfma(cf a, cf b, cf c) {
    return {__builtin_fmaf(a.r, b.r, __builtin_fmaf(-a.i, b.i, c.r)), __builtin_fmaf(a.r, b.i, __builtin_fmaf(a.i, b.r, c.i))};
}
DEVI void sincos_cw(float x, float& sn, float& cs) {
    const float q = __builtin_rintf(x * 0.636619772f);
    float r = __builtin_fmaf(q, -1.57079601e+00f, x);
    r = __builtin_fmaf(q, -3.13916473e-07f, r);
    r = __builtin_fmaf(q, -5.39030253e-15f, r);
    const float z = r * r;
    float ps = __builtin_fmaf(z, 2.86567956e-6f, -1.98559923e-4f);
    ps = __builtin_fmaf(z, ps, 8.33338592e-3f);
    ps = __builtin_fmaf(z, ps, -1.66666672e-1f);
    const float s0 = __builtin_fmaf(r * z, ps, r);
    float pc = __builtin_fmaf(z, 2.44677067e-5f, -1.38877297e-3f);
    pc = __builtin_fmaf(z, pc, 4.16666567e-2f);
    pc = __builtin_fmaf(z, pc, -0.5f);
    const float c0 = __builtin_fmaf(z, pc, 1.0f);
    const int n = (int)q;
    const float a = (n & 1) ? c0 : s0, b = (n & 1) ? s0 : c0;
    sn = (n & 2) ? -a : a;
    cs = ((n + 1) & 2) ? -b : b;
}
template <int NK, int WIN, class KS, class WF>
DEVI void phase2_loop(__amdgpu_buffer_rsrc_t hr, u32 voff, int nk, KS ksoff, WF wfrag, f32x16 (&acc)[2][2]) {
    u32x4 hf[WIN][2];
#pragma unroll
    for (int s = 0; s < WIN; ++s)
#pragma unroll
        for (int mt = 0; mt < 2; ++mt)
            hf[s][mt] = __builtin_amdgcn_raw_buffer_load_b128(hr, voff, ksoff(s) + mt * (NKS * 1024), 16);
    u32x4 wc0 = wfrag(0, 0), wc1 = wfrag(0, 1);
#pragma unroll
    for (int s = 0; s < NK; ++s) {
        u32x4 wn0 = wc0, wn1 = wc1;
        if (s + 1 < NK) { wn0 = wfrag(s + 1, 0); wn1 = wfrag(s + 1, 1); }
        __builtin_amdgcn_sched_barrier(0);
        if (s < NK - 1 || nk == NK) {
            acc[0][0] = mfma(hf[s % WIN][0], wc0, acc[0][0]);
            acc[0][1] = mfma(hf[s % WIN][0], wc1, acc[0][1]);
            acc[1][0] = mfma(hf[s % WIN][1], wc0, acc[1][0]);
            acc[1][1] = mfma(hf[s % WIN][1], wc1, acc[1][1]);
        }
        if (s + WIN < NK) {
#pragma unroll
            for (int mt = 0; mt < 2; ++mt)
                hf[s % WIN][mt] = __builtin_amdgcn_raw_buffer_load_b128(hr, voff, ksoff(s + WIN) + mt * (NKS * 1024), 16);
        }
        wc0 = wn0; wc1 = wn1;
        __builtin_amdgcn_sched_barrier(0);
    }
}

DEVI void poll_flags(const char* ws, u32 off, int lane, int lo, int hi, u64 tag) {
    const bool need = lane >= lo && lane <= hi;
    const GAS u64* p = (const GAS u64*)(ws + off + (need ? lane : lo) * 128);
    for (u32 spins = 0; spins < SPIN_LIMIT; ++spins) {
        u64 v = __hip_atomic_load(p, RLX_AGENT);
        if (__all(v == tag)) break;
        __builtin_amdgcn_s_sleep(1);
    }
}

DEVI u32 tag_a(u64 tag) { return (u32)tag; }
DEVI u32 tag_b(u64 tag) { return ((u32)tag * 0x9E3779B1u) ^ (u32)(tag >> 32); }
DEVI u32x4 granule(float v0, float v1, u64 tag) { return (u32x4){__float_as_uint(v0), tag_a(tag), __float_as_uint(v1), tag_b(tag)}; }
DEVI bool granule_ok(u32x4 g, u64 tag) { return g[1] == tag_a(tag) && g[3] == tag_b(tag); }
DEVI u64 read_tag(LAS char* ls, u32 scp) {
    const u32 e0 = *(LAS u32*)(ls + scp + 192), e1 = *(LAS u32*)(ls + scp + 196);
    return (((u64)__builtin_amdgcn_readfirstlane(e1) << 32) | __builtin_amdgcn_readfirstlane(e0)) + 1;
}
DEVI void stats_partial(u32x4 xr, float& s, float& q) {
    s = 0.f; q = 0.f;
#pragma unroll
    for (int d = 0; d < 4; ++d) {
        const float a = lo16(xr[d]), b = hi16(xr[d]);
        s += a; s += b;
        q = __builtin_fmaf(a, a, q); q = __builtin_fmaf(b, b, q);
    }
}
DEVI void stats_publish(__amdgpu_buffer_rsrc_t sr, int token, int lane, float s, float q, u64 tag) {
    const float mean = s * (1.0f / 512.0f);
    const float var = fmaxf(q * (1.0f / 512.0f) - mean * mean, 0.0f);
    if (lane == 63) {
        __builtin_amdgcn_raw_buffer_store_b128(granule(mean, var, tag), sr, token * 16, 0, 16);
    }
}
DEVI void stats_fetch(LAS char* ls, __amdgpu_buffer_rsrc_t sr, int tile_tok, int tok0, u64 tag, float& mean, float& var) {
    u32x4 g = lds128(ls, L_STG + tile_tok * 16);
    for (u32 spins = 0; spins < SPIN_LIMIT; ++spins) {
        if (__all(granule_ok(g, tag))) break;
        asm volatile("" ::: "memory");
        g = __builtin_amdgcn_raw_buffer_load_b128(sr, (tok0 + tile_tok) * 16, 0, 16);
    }
    mean = __uint_as_float(g[0]);
    var = __uint_as_float(g[2]);
}
DEVI u32 split2(float v) {
    const u32 h = pack2(v, v);
    return (h & 0xffffu) | (pack2(v - lo16(h), 0.f) << 16);
}

__global__ __launch_bounds__(512, 2) void s5_mlp_fused(
    const u16* __restrict__ x, const u16* __restrict__ Lre, const u16* __restrict__ Lim, const u16* __restrict__ Bre,
    const u16* __restrict__ Bim, const u16* __restrict__ Cre, const u16* __restrict__ Cim, const u16* __restrict__ ldt,
    const u16* __restrict__ Dsk, const u16* __restrict__ w1, const u16* __restrict__ b1, const u16* __restrict__ w2,
    const u16* __restrict__ b2, float* __restrict__ out, char* __restrict__ ws) {
    extern __shared__ __attribute__((aligned(16))) char smem[];
    LAS char* ls = (LAS char*)smem;
    const int blk = blockIdx.x;
    const int xg = blk & 7, rr = blk >> 3;
    const int ti = 4 * (xg >> 1) + (rr >> 3);
    const int gj = 8 * (xg & 1) + (rr & 7);
    const int wave = __builtin_amdgcn_readfirstlane(threadIdx.x >> 6);
    const int lane = threadIdx.x & 63;
    const int l31 = lane & 31, hh = lane >> 5;
    const int ntile = gj < 11 ? 3 : 2;
    const int f0 = gj < 11 ? 96 * gj : 1056 + 64 * (gj - 11);
    const int n0 = 64 * (gj & 7);
    const int Tp2 = 4 * ti + 2 * (gj >> 3);
    const int wq = wave & 3;

    const __amdgpu_buffer_rsrc_t hr = __builtin_amdgcn_make_buffer_rsrc(ws + WS_H, 0, H_BYTES, 0x00020000);
    const __amdgpu_buffer_rsrc_t gr = __builtin_amdgcn_make_buffer_rsrc(ws + WS_GRAN, 0, 131072, 0x00020000);
    const __amdgpu_buffer_rsrc_t orr = __builtin_amdgcn_make_buffer_rsrc(out, 0, 2048 * 512 * 4, 0x00020000);
    const __amdgpu_buffer_rsrc_t sr = __builtin_amdgcn_make_buffer_rsrc(ws + WS_STAT, 0, 32768, 0x00020000);
    const int stok = 128 * ti + 8 * gj + wave;
    const u32 scr = A_X + 3 * SL_X + wave * 1024, scp = A_X + 3 * SL_X + 8192 + wave * 256;
    dma16((const char*)x + (size_t)stok * 1024, (threadIdx.x & 63) * 16, scr);
    {
        const char* p = lane < 16 ? (const char*)Lre + 64 * gj + 4 * lane
                      : lane < 32 ? (const char*)Lim + 64 * gj + 4 * (lane - 16)
                      : lane < 48 ? (const char*)ldt + 64 * gj + 4 * (lane - 32)
                                  : (const char*)ws + WS_EPOCH + 4 * (lane & 1);
        dma4v(p, scp);
    }

    const u32 a0 = l31 * 128 + ((hh ^ ((lane >> 1) & 7)) << 4);
    u32 ar[4];
#pragma unroll
    for (int s = 0; s < 4; ++s) ar[s] = a0 ^ (((s + wq) & 3) << 5);
    const u32 sw_e = ((lane & 7) ^ (lane >> 4)) << 4, sw_o = ((lane & 7) ^ (4 + (lane >> 4))) << 4;
    const u32 rowoff = (lane >> 3) * 1024;

    const char* code_base;
    u32 code_len;
    asm volatile("s_getpc_b64 %0\n.Lcode_mark0:\n\ts_mov_b32 %1, .Lcode_mark1-.Lcode_mark0" : "=s"(code_base), "=s"(code_len));

    auto warm_code = [&]() {
        const int ln = opaque(lane);
        dma4v(code_base + min((u32)((rr * 10 + ln % 10) * 128), code_len - 4u), L_JUNK);
    };
    auto slow_off = [&](const void* p) { return (u32)((384u - ((u32)(size_t)p & 1023u)) & 1023u & ~127u); };
    auto warm_slow = [&](int part) {
        const int ln = opaque(lane);
        const char* q;
        if (part == 0) q = (const char*)w2 + (size_t)(43 * rr + min(ln, 42)) * 1024 + slow_off(w2);
        else q = (const char*)(ln < 32 ? Cre : Cim) + (size_t)(16 * rr + (ln & 15)) * 1024 + slow_off(ln < 32 ? Cre : Cim);
        dma4v(q, L_JUNK);
    };
    auto warm_w2 = [&](int part) {
        const int ln = opaque(lane);
        const char* q = part == 0 ? (const char*)w2 + (size_t)blk * 5504 + min(ln, 42) * 128
                                  : (const char*)(ln < 32 ? Cre : Cim) + blk * 2048 + (ln & 15) * 128;
        dma4v(q, L_JUNK);
    };

    if (threadIdx.x < 8) *(LAS u32*)(ls + L_CNT + threadIdx.x * 4) = 0;

    f32x16 acc2[2][2];
    u64 tag;
    const int otok = 64 * (gj >> 3) + 8 * wave + (lane >> 4);
    const int ocol = n0 + 4 * (lane & 15);
    u32x2 xv[2];
    auto load_x = [&](u32x2 (&v)[2]) {
#pragma unroll
        for (int e = 0; e < 2; ++e) v[e] = *(const u32x2*)(x + (size_t)(128 * ti + otok + 4 * e) * 512 + ocol);
    };

    if (wave < 4) {
        const int w = wq;
        const char* xb = (const char*)x + (size_t)(128 * ti + 32 * w) * 1024;
        const u32 ve = rowoff + sw_e, vo = rowoff + 8192 + sw_o;
        const char* bb = (const char*)(w < 2 ? Bre : Bim) + (size_t)(32 * gj + 16 * (w & 1)) * 1024;
        u32 vw[3];
#pragma unroll
        for (int q = 0; q < 3; ++q) {
            const int u = 64 * (3 * w + q) + lane, k = u / 12, cc = u - 12 * k;
            vw[q] = k * (FF * 2) + f0 * 2 + ((ntile == 2 && cc >= 8) ? cc - 8 : cc) * 16;
        }
        auto issue1 = [&](int c, int q) {
            if (q < 4) dma16(xb + c * 128 + (q >> 1) * 16384, (q & 1) ? vo : ve, A_X + (c & 3) * SL_X + w * 4096 + q * 1024);
            else if (q < 6) dma16(bb + c * 128, (q & 1) ? vo : ve, A_B + (c & 3) * SL_B + w * 2048 + (q - 4) * 1024);
            else dma16((const char*)w1 + (size_t)c * (64 * FF * 2), vw[q - 6], A_W + (c & 3) * SL_W + (3 * w + q - 6) * 1024);
        };
#pragma unroll
        for (int c = 0; c < 2; ++c)
#pragma unroll
            for (int q = 0; q < 4; ++q) issue1(c, q);

        u32 ax[4], ab[4];
#pragma unroll
        for (int s = 0; s < 4; ++s) { ax[s] = A_X + w * 4096 + ar[s]; ab[s] = A_B + ar[s]; }
        wait_vm<8>();
        cf L1, L2, L3, L4, L32, L128, L256, Lw, c2;
        {
            const u32x4 xr = lds128(ls, scr + lane * 16);
            const float lr = bf2f(*(LAS u16*)(ls + scp + l31 * 2));
            const float li = bf2f(*(LAS u16*)(ls + scp + 64 + l31 * 2));
            const float ld = bf2f(*(LAS u16*)(ls + scp + 128 + l31 * 2));
            tag = read_tag(ls, scp);
            float st_s, st_q;
            stats_partial(xr, st_s, st_q);
            stats_publish(sr, stok, lane, wave_sum63(st_s), wave_sum63(st_q), tag);
#pragma unroll
            for (int q = 0; q < 4; ++q) issue1(2, q);
            const float dtv = (float)(__bf16)__builtin_amdgcn_exp2f(ld * 1.4426950409f);
            const float mag = __builtin_amdgcn_exp2f(dtv * lr * 1.4426950409f);
            float sn, cs;
            sincos_cw(dtv * li, sn, cs);
            L1 = {mag * cs, mag * sn};
            const float nr = L1.r - 1.0f, ni = L1.i, den = 2.0f * __builtin_amdgcn_rcpf(lr * lr + li * li);
            c2 = {(nr * lr + ni * li) * den, (ni * lr - nr * li) * den};
            L2 = cmul(L1, L1); L3 = cmul(L2, L1); L4 = cmul(L2, L2);
            cf t = cmul(L4, L4); t = cmul(t, t); L32 = cmul(t, t);
            const cf L64 = cmul(L32, L32);
            L128 = cmul(L64, L64); L256 = cmul(L128, L128);
            Lw = w == 0 ? cf{1.f, 0.f} : (w == 1 ? L32 : (w == 2 ? L64 : cmul(L64, L32)));
            PIN(L1.r); PIN(L1.i); PIN(L2.r); PIN(L2.i); PIN(L3.r); PIN(L3.i); PIN(L4.r); PIN(L4.i); PIN(L32.r); PIN(L32.i);
            PIN(L128.r); PIN(L128.i); PIN(L256.r); PIN(L256.i); PIN(Lw.r); PIN(Lw.i); PIN(c2.r); PIN(c2.i);
        }
        f32x16 are = {}, aim = {};
        float csr = 0.f, csi = 0.f;
        u32x4 fx[2], fr[2], fi[2];
        wait_vm_barrier<9>();
        fx[0] = lds128(ls, ax[0]); fr[0] = lds128(ls, ab[0]); fi[0] = lds128(ls, ab[0] + 4096);
        SB();
#pragma unroll
        for (int u = 0; u < 32; ++u) {
            const int c = u >> 2, s = u & 3, cn = (u + 1) >> 2, sn_ = (u + 1) & 3;
            are = mfma(fx[u & 1], fr[u & 1], are);
            if (s == 3 && c < 7) {
                if (c == 0) wait_vm_barrier<12>();
                else if (c <= 4) wait_vm_barrier<16>();
                else if (c == 5) wait_vm_barrier<10>();
                else wait_vm_barrier<1>();
            }
            SB();
            if (u < 31) {
                fx[(u + 1) & 1] = lds128(ls, ax[sn_] + (cn & 3) * SL_X);
                fr[(u + 1) & 1] = lds128(ls, ab[sn_] + (cn & 3) * SL_B);
            }
            if (c <= 4) issue1(c + 3, s == 0 ? 0 : 2 * s + 1);
            csr = dsum(fr[u & 1][s], csr);
            PIN(csr);
            SB();
            aim = mfma(fx[u & 1], fi[u & 1], aim);
            SB();
            if (u < 31) fi[(u + 1) & 1] = lds128(ls, ab[sn_] + (cn & 3) * SL_B + 4096);
            if (c <= 4) { issue1(c + 3, s == 0 ? 1 : 2 * s + 2); if (s == 0) issue1(c + 3, 2); }
            if (u == 20 && hh == 0) dma16c(ws + WS_STAT + (128 * ti + 32 * w) * 16, l31 * 16, L_STG + w * 512);
            csi = dsum(fi[u & 1][s], csi);
            PIN(csi);
            SB();
        }
        csr = both_halves_sum(csr);
        csi = both_halves_sum(csi);
        *(LAS float*)(ls + L_PS + (64 * w + lane) * 4) = hh ? csi : csr;
        wait_vm<0>();
        barrier_lds();

        const u32 reg = w * REG_S;
        const int lo_ = opaque(lane);
        const u32 cvb = (lo_ >> 3) * 1024 + n0 * 1024 + w * 256;
        const u32 cve = cvb + (((lo_ & 7) ^ (lo_ >> 4)) << 4), cvo = cvb + 8192 + (((lo_ & 7) ^ (4 + (lo_ >> 4))) << 4);
        auto issueC = [&](const u16* C) {
#pragma unroll
            for (int U = 0; U < 2; ++U)
#pragma unroll
                for (int n = 0; n < 4; ++n) {
                    dma16((const char*)C + U * 128 + n * 16384, cve, reg + U * 8192 + n * 2048);
                    dma16((const char*)C + U * 128 + n * 16384, cvo, reg + U * 8192 + n * 2048 + 1024);
                }
        };

        float er[16], ei[16];
        {
            float ps[8];
#pragma unroll
            for (int q = 0; q < 4; ++q) {
                ps[q] = ldsf(ls, L_PS + (64 * q + l31) * 4);
                ps[4 + q] = ldsf(ls, L_PS + (64 * q + 32 + l31) * 4);
            }
            float mean, var;
            stats_fetch(ls, sr, 32 * w + l31, 128 * ti, tag, mean, var);
            const float r1 = __builtin_amdgcn_rsqf(var + 1e-5f);
            *(LAS float*)(ls + (hh ? L_RM : L_R1) + (32 * w + l31) * 4) = hh ? r1 * mean : r1;
            const float sbr = (ps[0] + ps[1]) + (ps[2] + ps[3]), sbi = (ps[4] + ps[5]) + (ps[6] + ps[7]);
            const u32 nm = split2(-mean), sr2 = split2(sbr), si2 = split2(sbi);
            const u32 z = hh ? 0u : 0xffffffffu;
            const u32x4 ea = {nm & z, nm & z & 0xffffu, 0u, 0u};
            const u32x4 ebr = {((sr2 & 0xffffu) | (sr2 << 16)) & z, (sr2 >> 16) & z, 0u, 0u};
            const u32x4 ebi = {((si2 & 0xffffu) | (si2 << 16)) & z, (si2 >> 16) & z, 0u, 0u};
            are = mfma(ea, ebr, are);
            aim = mfma(ea, ebi, aim);
            wait_lgkm();
            f32x4 r1q[4];
#pragma unroll
            for (int g = 0; g < 4; ++g) r1q[g] = ldsf4(ls, L_R1 + (32 * w + 8 * g + 4 * hh) * 4);
            SB();
#pragma unroll
            for (int r = 0; r < 16; ++r) { er[r] = are[r] * r1q[r >> 2][r & 3]; ei[r] = aim[r] * r1q[r >> 2][r & 3]; }
            PIN16(er); PIN16(ei);
        }
#pragma unroll
        for (int q = 1; q < 4; ++q) {
            float tr[4], tq[4];
#pragma unroll
            for (int g = 0; g < 4; ++g) {
                tr[g] = __builtin_fmaf(-L1.i, ei[4 * g + q - 1], er[4 * g + q]);
                tq[g] = __builtin_fmaf(L1.i, er[4 * g + q - 1], ei[4 * g + q]);
            }
            asm volatile("" : "+v"(tr[0]), "+v"(tr[1]), "+v"(tr[2]), "+v"(tr[3]), "+v"(tq[0]), "+v"(tq[1]), "+v"(tq[2]), "+v"(tq[3]));
#pragma unroll
            for (int g = 0; g < 4; ++g) {
                er[4 * g + q] = __builtin_fmaf(L1.r, er[4 * g + q - 1], tr[g]);
                ei[4 * g + q] = __builtin_fmaf(L1.r, ei[4 * g + q - 1], tq[g]);
            }
            asm volatile("" : "+v"(er[q]), "+v"(er[4 + q]), "+v"(er[8 + q]), "+v"(er[12 + q]), "+v"(ei[q]), "+v"(ei[4 + q]), "+v"(ei[8 + q]), "+v"(ei[12 + q]));
        }
        cf ag[8];
#pragma unroll
        for (int g = 0; g < 4; ++g) {
            halves(er[4 * g + 3], ag[2 * g].r, ag[2 * g + 1].r);
            halves(ei[4 * g + 3], ag[2 * g].i, ag[2 * g + 1].i);
        }
        cf wag = ag[0];
#pragma unroll
        for (int q = 1; q < 8; ++q) wag = cfma(L4, wag, ag[q]);
        if (hh == 0) *(LAS f32x2*)(ls + L_WAG + (32 * w + l31) * 8) = (f32x2){wag.r, wag.i};
        wait_lgkm();
        if (lane == 0) __hip_atomic_store((LAS u32*)(ls + L_CNT + w * 4), 1u, RLX_WG);

        cf pre = {0.f, 0.f};
        if (w > 0) {
            for (u32 spins = 0; spins < SPIN_LIMIT; ++spins) {
                u32 v = __hip_atomic_load((LAS u32*)(ls + L_CNT + (lane < w ? lane : 0) * 4), RLX_WG);
                if (__all(v == 1u)) break;
            }
            asm volatile("" ::: "memory");
            for (int q = 0; q < w; ++q) {
                const f32x2 t = *(LAS f32x2*)(ls + L_WAG + (32 * q + l31) * 8);
                pre = cfma(L32, pre, cf{t.x, t.y});
            }
        }
        if (w == 3 && ti < 15) {
            const cf sb = cfma(L32, pre, wag);
            if (hh == 0) __builtin_amdgcn_raw_buffer_store_b128(granule(sb.r, sb.i, tag), gr, ((gj * 16 + ti) * 32 + l31) * 16, 0, 16);
        }
        issueC(Cre);
        cf Fq[4];
        Fq[0] = cmul(c2, L1); Fq[1] = cmul(c2, L2); Fq[2] = cmul(c2, L3); Fq[3] = cmul(c2, L4);
#pragma unroll
        for (int r = 0; r < 16; ++r) {
            const float a = er[r], b = ei[r];
            er[r] = c2.r * a - c2.i * b;
            ei[r] = -(c2.r * b + c2.i * a);
        }
        PIN16(er); PIN16(ei);
        u32x4 wr[8][2];
        auto fetch_cre = [&]() {
            wait_vm<0>();
#pragma unroll
            for (int s = 0; s < 8; ++s)
#pragma unroll
                for (int nt = 0; nt < 2; ++nt) wr[s][nt] = lds128(ls, reg + (s >> 2) * 8192 + nt * 4096 + (a0 ^ ((s & 3) << 5)));
            wait_lgkm();
        };
        if (w != 0 || ti == 0) fetch_cre();

        cf cin = {0.f, 0.f};
        if (ti > 0) {
            if (w == 0) {
                __builtin_amdgcn_s_sleep(4);
                u32x4 gv[8];
                for (u32 spins = 0; spins < SPIN_LIMIT; ++spins) {
                    asm volatile("" ::: "memory");
#pragma unroll
                    for (int q = 0; q < 8; ++q) {
                        const int ip = 2 * q + hh;
                        const int ic = ip < ti ? ip : 0;
                        gv[q] = __builtin_amdgcn_raw_buffer_load_b128(gr, ((gj * 16 + ic) * 32 + l31) * 16, 0, 16);
                    }
                    PIN8(gv, 0);
                    bool ok = true;
#pragma unroll
                    for (int q = 0; q < 8; ++q) ok = ok && granule_ok(gv[q], tag);
                    if (__all(ok)) break;
                    __builtin_amdgcn_s_sleep(2);
                }
                cf A = {0.f, 0.f};
#pragma unroll
                for (int q = 0; q < 8; ++q) {
                    const cf t = cfma(L256, A, cf{__uint_as_float(gv[q][0]), __uint_as_float(gv[q][2])});
                    const bool v = 2 * q + hh < ti;
                    A.r = v ? t.r : A.r; A.i = v ? t.i : A.i;
                }
                const bool last = ((ti - 1) & 1) == hh;
                const cf B = cmul(L128, A);
                A.r = last ? A.r : B.r; A.i = last ? A.i : B.i;
                cin = {both_halves_sum(A.r), both_halves_sum(A.i)};
                if (hh == 0) *(LAS f32x2*)(ls + L_CIN + l31 * 8) = (f32x2){cin.r, cin.i};
                wait_lgkm();
                if (lane == 0) __hip_atomic_store((LAS u32*)(ls + L_CNT + 24), 1u, RLX_WG);
                fetch_cre();
            } else {
                for (u32 spins = 0; spins < SPIN_LIMIT; ++spins) {
                    if (__hip_atomic_load((LAS u32*)(ls + L_CNT + 24), RLX_WG) == 1u) break;
                    __builtin_amdgcn_s_sleep(1);
                }
                asm volatile("" ::: "memory");
                const f32x2 t = *(LAS f32x2*)(ls + L_CIN + l31 * 8);
                cin = {t.x, t.y};
            }
        }
        cf Q = cfma(Lw, cin, pre);
        cf qs[4];
#pragma unroll
        for (int g = 0; g < 4; ++g) {
            const cf Qe = Q;
            const cf Qo = cfma(L4, Qe, ag[2 * g]);
            Q = cfma(L4, Qo, ag[2 * g + 1]);
            qs[g].r = hh ? Qo.r : Qe.r; qs[g].i = hh ? Qo.i : Qe.i;
        }
#pragma unroll
        for (int g = 0; g < 4; ++g) {
            float hr_[4], hi_[4];
#pragma unroll
            for (int q = 0; q < 4; ++q) {
                hr_[q] = __builtin_fmaf(Fq[q].r, qs[g].r, __builtin_fmaf(-Fq[q].i, qs[g].i, er[4 * g + q]));
                hi_[q] = __builtin_fmaf(-Fq[q].r, qs[g].i, __builtin_fmaf(-Fq[q].i, qs[g].r, ei[4 * g + q]));
            }
            const u32 o = reg + l31 * 64 + ((g ^ ((l31 >> 1) & 3)) << 4) + 8 * hh;
            *(LAS u32x2*)(ls + o) = (u32x2){pack2(hr_[0], hr_[1]), pack2(hr_[2], hr_[3])};
            *(LAS u32x2*)(ls + o + 2048) = (u32x2){pack2(hi_[0], hi_[1]), pack2(hi_[2], hi_[3])};
        }
        wait_lgkm();
        {
            const int G = lane >> 4, q4 = (lane & 15) >> 2, p4 = lane & 3;
            const int tq = 4 * (G & 1) + p4;
#pragma unroll
            for (int kk = 0; kk < 4; ++kk) {
                const int r0 = (kk >> 1) * 32 + (kk & 1) * 16 + 8 * hh + q4;
                const u32 p0 = reg + r0 * 64 + (((tq >> 1) ^ ((r0 >> 1) & 3)) << 4) + 8 * (tq & 1);
                const u32 p1 = reg + (r0 + 4) * 64 + (((tq >> 1) ^ (((r0 + 4) >> 1) & 3)) << 4) + 8 * (tq & 1);
                const u32x4 fr = ldstr(ls, p0, p1);
                __builtin_amdgcn_raw_buffer_store_b128(fr, hr, lane * 16 + ((4 * ti + w) * NKS + 4 * gj + kk) * 1024, 0, 16);
            }
        }
        wait_lgkm();
        wait_vm<0>();
        {
            u32 old = 0;
            if (lane == 0) old = __hip_atomic_fetch_add((LAS u32*)(ls + L_CNT + 20), 1u, RLX_WG);
            if (__builtin_amdgcn_readfirstlane(old) == 3 && lane == 0)
                __hip_atomic_store((GAS u64*)(ws + WS_FLAGS + (ti * 16 + gj) * 128), tag, RLX_AGENT);
        }
        issueC(Cim);
        load_x(xv);
        poll_flags(ws, WS_FLAGS + ti * 2048, lane, 4 * w, 4 * w + 3, tag);
        wait_vm<0>();
#pragma unroll
        for (int a = 0; a < 2; ++a)
#pragma unroll
            for (int b = 0; b < 2; ++b) acc2[a][b] = (f32x16){};
        const u32 hv = lane * 16 + (Tp2 * NKS + 16 * w) * 1024;
        phase2_loop<16, SWIN>(hr, hv, 16,
            [&](int s) { return (u32)((4 * ((s & 7) >> 1) + 2 * (s >> 3) + (s & 1)) * 1024); },
            [&](int s, int nt) { return s < 8 ? wr[s & 7][nt] : lds128(ls, reg + ((s & 7) >> 2) * 8192 + nt * 4096 + (a0 ^ ((s & 3) << 5))); },
            acc2);
    } else {
        const int m = wq;
        if (m == 3) warm_code();
        if (m == 1) {
            dma4v((const char*)b1 + f0 * 2 + 4 * min(lane, ntile * 16 - 1), L_B1R);
        } else if (m == 2) {
            dma4v((lane < 32 ? (const char*)Dsk : (const char*)b2) + n0 * 2 + 4 * l31, L_DB);
        }
        const char* bb = (const char*)(m < 2 ? Bre : Bim) + (size_t)(32 * gj + 16 * (m & 1)) * 1024;
        const u32 ve = rowoff + sw_e, vo = rowoff + 8192 + sw_o;
        u32 vw[3];
#pragma unroll
        for (int q = 0; q < 3; ++q) {
            const int u = 64 * (3 * m + q) + lane, k = u / 12, cc = u - 12 * k;
            vw[q] = k * (FF * 2) + f0 * 2 + ((ntile == 2 && cc >= 8) ? cc - 8 : cc) * 16;
        }
        auto issue1 = [&](int c, int q) {
            if (q < 2) dma16(bb + c * 128, q ? vo : ve, A_B + (c & 3) * SL_B + m * 2048 + q * 1024);
            else dma16((const char*)w1 + (size_t)c * (64 * FF * 2), vw[q - 2], A_W + (c & 3) * SL_W + (3 * m + q - 2) * 1024);
        };
#pragma unroll
        for (int c = 0; c < 2; ++c)
#pragma unroll
            for (int q = 0; q < 5; ++q) issue1(c, q);

        const int G = lane >> 4, q4 = (lane & 15) >> 2, p4 = lane & 3;
        LAS char* tw[4];
        u32 ax[4];
#pragma unroll
        for (int s = 0; s < 4; ++s) {
            tw[s] = ls + (A_W + (16 * ((s + m) & 3) + 8 * hh + q4) * 192 + 32 * (G & 1) + 8 * p4);
            asm volatile("" : "+v"(tw[s]));
            ax[s] = A_X + m * 4096 + ar[s];
        }
        wait_vm<10>();
        {
            const u32x4 xr = lds128(ls, scr + lane * 16);
            tag = read_tag(ls, scp);
            float st_s, st_q;
            stats_partial(xr, st_s, st_q);
            stats_publish(sr, stok, lane, wave_sum63(st_s), wave_sum63(st_q), tag);
#pragma unroll
            for (int q = 0; q < 5; ++q) issue1(2, q);
        }
        f32x16 am[3] = {};
        float cs[3] = {0.f, 0.f, 0.f};
        u32x4 fx[2], fw[2][3];
        auto rdw = [&](int u, int n) {
            const int c = u >> 2, s = u & 3;
            fw[u & 1][n] = ldstrp(tw[s], (c & 3) * SL_W + n * 64, (c & 3) * SL_W + n * 64 + 768);
        };
        wait_vm_barrier<11>();
        fx[0] = lds128(ls, ax[0]); rdw(0, 0); rdw(0, 1); rdw(0, 2);
        SB();
#pragma unroll
        for (int u = 0; u < 32; ++u) {
            const int c = u >> 2, s = u & 3, cn = (u + 1) >> 2, sn_ = (u + 1) & 3;
            am[0] = mfma(fw[u & 1][0], fx[u & 1], am[0]);
            if (s == 3 && c < 7) {
                if (c == 0) wait_vm_barrier<6>();
                else if (c == 1) wait_vm_barrier<0>();
                else barrier_lds();
            }
            SB();
            if (u < 31) { fx[(u + 1) & 1] = lds128(ls, ax[sn_] + (cn & 3) * SL_X); rdw(u + 1, 0); }
            cs[0] += lo16(fw[u & 1][0][s]); cs[0] += hi16(fw[u & 1][0][s]);
            PIN(cs[0]);
            SB();
            am[1] = mfma(fw[u & 1][1], fx[u & 1], am[1]);
            SB();
            if (u < 31) rdw(u + 1, 1);
            cs[1] += lo16(fw[u & 1][1][s]); cs[1] += hi16(fw[u & 1][1][s]);
            PIN(cs[1]);
            SB();
            am[2] = mfma(fw[u & 1][2], fx[u & 1], am[2]);
            SB();
            if (u < 31) rdw(u + 1, 2);
            if (u == WARM_U && m < 2) warm_w2(m);
            if (u == WARM_U + 2 && m >= 2) warm_slow(m - 2);
            cs[2] += lo16(fw[u & 1][2][s]); cs[2] += hi16(fw[u & 1][2][s]);
            PIN(cs[2]);
            SB();
        }
#pragma unroll
        for (int n = 0; n < 3; ++n) cs[n] = both_halves_sum(cs[n]);
        *(LAS float*)(ls + L_PM + (96 * m + lane) * 4) = hh ? cs[1] : cs[0];
        if (hh == 0) *(LAS float*)(ls + L_PM + (96 * m + 64 + l31) * 4) = cs[2];
        barrier_lds();

        const int kb = m < 2 ? 22 * m : 44 + 21 * (m - 2);
        const int nk = m < 2 ? 22 : 21;
        const u32 reg = REG_M0 + m * REG_M;
        const int lo_ = opaque(lane);
        const u32 wv = (lo_ >> 3) * 1024 + n0 * 2 + (((lo_ & 7) ^ (((lo_ >> 4) & 1) << 2)) << 4);
        auto issueW1 = [&](int part, int n) {
            const int row = min(16 * (kb + 11 * part) + 8 * n, FF - 8);
            dma16((const char*)w2 + (size_t)row * 1024, wv, reg + n * 1024);
        };
        float R;
        {
            float ps[3][4];
            u32 bv[3];
#pragma unroll
            for (int n = 0; n < 3; ++n) {
#pragma unroll
                for (int q = 0; q < 4; ++q) ps[n][q] = ldsf(ls, L_PM + (96 * q + 32 * n + l31) * 4);
                bv[n] = *(LAS u16*)(ls + L_B1R + (32 * n + l31) * 2);
            }
            float mean, var;
            stats_fetch(ls, sr, 32 * m + l31, 128 * ti, tag, mean, var);
            const float r1 = __builtin_amdgcn_rsqf(var + 1e-5f);
            const float r2 = __builtin_amdgcn_rsqf(var * r1 * r1 + 1e-5f);
            R = r1 * r2;
            const u32 nm = split2(-mean), ir = split2(__builtin_amdgcn_rcpf(R));
            const u32 z = hh ? 0u : 0xffffffffu;
            const u32x4 eb = {nm & z, ((nm & 0xffffu) | (ir << 16)) & z, (ir >> 16) & z, 0u};
#pragma unroll
            for (int n = 0; n < 3; ++n) {
                const u32 s2 = split2((ps[n][0] + ps[n][1]) + (ps[n][2] + ps[n][3]));
                const u32x4 ea = {((s2 & 0xffffu) | (s2 << 16)) & z, ((s2 >> 16) | (bv[n] << 16)) & z, bv[n] & z, 0u};
                am[n] = mfma(ea, eb, am[n]);
            }
        }
        u32x4 hfr[3][2];
        auto dmag = [&](int q) { if (q < 22) issueW1(0, q); };
#pragma unroll
        for (int n = 0; n < 3; ++n) {
            f32x2 zv[8], tv[8];
            const f32x2 R2 = {R, R}, c1 = {-0.1029432397f, -0.1029432397f}, c0 = {-2.3022081985f, -2.3022081985f}, one = {1.0f, 1.0f};
#pragma unroll
            for (int r = 0; r < 8; ++r) zv[r] = (f32x2){am[n][2 * r], am[n][2 * r + 1]} * R2;
            PIN8(zv, 0);
            dmag(8 * n);
#pragma unroll
            for (int r = 0; r < 8; ++r) tv[r] = zv[r] * zv[r];
            PIN8(tv, 0);
            dmag(8 * n + 1);
#pragma unroll
            for (int r = 0; r < 8; ++r) tv[r] = __builtin_elementwise_fma(tv[r], c1, c0);
            PIN8(tv, 0);
            dmag(8 * n + 2);
#pragma unroll
            for (int r = 0; r < 8; ++r) tv[r] = tv[r] * zv[r];
            PIN8(tv, 0);
            dmag(8 * n + 3);
#pragma unroll
            for (int r = 0; r < 8; ++r) tv[r] = (f32x2){__builtin_amdgcn_exp2f(tv[r].x), __builtin_amdgcn_exp2f(tv[r].y)};
            PIN8(tv, 0);
            dmag(8 * n + 4);
#pragma unroll
            for (int r = 0; r < 8; ++r) tv[r] = tv[r] + one;
            PIN8(tv, 0);
            dmag(8 * n + 5);
#pragma unroll
            for (int r = 0; r < 8; ++r) tv[r] = (f32x2){__builtin_amdgcn_rcpf(tv[r].x), __builtin_amdgcn_rcpf(tv[r].y)};
            PIN8(tv, 0);
            dmag(8 * n + 6);
#pragma unroll
            for (int r = 0; r < 8; ++r) tv[r] = tv[r] * zv[r];
            PIN8(tv, 0);
            dmag(8 * n + 7);
#pragma unroll
            for (int s = 0; s < 2; ++s) {
                hfr[n][s] = (u32x4){pack2(tv[4 * s].x, tv[4 * s].y), pack2(tv[4 * s + 1].x, tv[4 * s + 1].y),
                                    pack2(tv[4 * s + 2].x, tv[4 * s + 2].y), pack2(tv[4 * s + 3].x, tv[4 * s + 3].y)};
                PIN(hfr[n][s]);
            }
        }
#pragma unroll
        for (int n = 0; n < 3; ++n)
            if (n < 2 || ntile == 3) {
#pragma unroll
                for (int s = 0; s < 2; ++s)
                    __builtin_amdgcn_raw_buffer_store_b128(hfr[n][s], hr, lane * 16 + ((4 * ti + m) * NKS + 64 + f0 / 16 + 2 * n + s) * 1024, 0, 16);
            }
        if (ntile == 3) wait_vm<6>(); else wait_vm<4>();
        const u32 t0a = reg + (4 * hh + q4) * 128 + (((2 * (G & 1) + (p4 >> 1)) ^ ((q4 >> 1) << 2)) << 4) + 8 * (p4 & 1);
        const u32 t1a = t0a ^ 64;
        LAS char* pt0 = ls + t0a;
        LAS char* pt1 = ls + t1a;
        asm volatile("" : "+v"(pt0), "+v"(pt1));
        u32x4 wr[11][2];
#pragma unroll
        for (int s = 0; s < 11; ++s) {
            wr[s][0] = ldstrp(pt0, s * 2048, s * 2048 + 1024);
            wr[s][1] = ldstrp(pt1, s * 2048, s * 2048 + 1024);
        }
        wait_lgkm();
        wait_vm<0>();
        {
            u32 old = 0;
            if (lane == 0) old = __hip_atomic_fetch_add((LAS u32*)(ls + L_CNT + 16), 1u, RLX_WG);
            if (__builtin_amdgcn_readfirstlane(old) == 3 && lane == 0)
                __hip_atomic_store((GAS u64*)(ws + WS_FLAGM + (ti * 16 + gj) * 128), tag, RLX_AGENT);
        }
#pragma unroll
        for (int n = 0; n < 22; ++n) issueW1(1, n);
        load_x(xv);
        poll_flags(ws, WS_FLAGM + ti * 2048, lane, 0, 15, tag);
        wait_vm<0>();
#pragma unroll
        for (int a = 0; a < 2; ++a)
#pragma unroll
            for (int b = 0; b < 2; ++b) acc2[a][b] = (f32x16){};
        const u32 hv = lane * 16 + (Tp2 * NKS + 64 + kb) * 1024;
        phase2_loop<22, MWIN>(hr, hv, nk,
            [&](int s) { return (u32)(s * 1024); },
            [&](int s, int nt) {
                return s < 11 ? wr[s < 11 ? s : 0][nt]
                              : ldstrp(nt ? pt1 : pt0, (s - 11) * 2048, (s - 11) * 2048 + 1024);
            },
            acc2);
    }

    {
        const u32 myreg = wave < 4 ? wave * REG_S : REG_M0 + (wave - 4) * REG_M;
#pragma unroll
        for (int mt = 0; mt < 2; ++mt)
#pragma unroll
            for (int nt = 0; nt < 2; ++nt)
#pragma unroll
                for (int r = 0; r < 16; ++r)
                    *(LAS float*)(ls + myreg + ((32 * mt + 8 * (r >> 2) + 4 * hh + (r & 3)) * 64 + 32 * nt + l31) * 4) = acc2[mt][nt][r];
        barrier_lds();
        if (ti == 15 && gj == 0 && threadIdx.x == 0) __hip_atomic_store((GAS u64*)(ws + WS_EPOCH), tag, RLX_AGENT);
        f32x4 own[2] = {};
#pragma unroll
        for (int u = 0; u < 8; ++u) {
            const u32 ureg = u < 4 ? u * REG_S : REG_M0 + (u - 4) * REG_M;
#pragma unroll
            for (int e = 0; e < 2; ++e) own[e] += ldsf4(ls, ureg + ((8 * wave + (lane >> 4) + 4 * e) * 64 + 4 * (lane & 15)) * 4);
        }
        const u32x2 dq = *(LAS u32x2*)(ls + L_DB + (lane & 15) * 8), bq = *(LAS u32x2*)(ls + L_DB + 128 + (lane & 15) * 8);
        const float dsk[4] = {lo16(dq.x), hi16(dq.x), lo16(dq.y), hi16(dq.y)};
        const float bb2[4] = {lo16(bq.x), hi16(bq.x), lo16(bq.y), hi16(bq.y)};
#pragma unroll
        for (int e = 0; e < 2; ++e) {
            const float r1 = ldsf(ls, L_R1 + (otok + 4 * e) * 4), rm = ldsf(ls, L_RM + (otok + 4 * e) * 4);
            const float xf[4] = {lo16(xv[e].x), hi16(xv[e].x), lo16(xv[e].y), hi16(xv[e].y)};
            u32x4 o;
#pragma unroll
            for (int q = 0; q < 4; ++q)
                o[q] = __float_as_uint(own[e][q] + xf[q] + __builtin_fmaf(__builtin_fmaf(xf[q], r1, -rm), dsk[q], bb2[q]));
            __builtin_amdgcn_raw_buffer_store_b128(o, orr, ((128 * ti + otok + 4 * e) * 512 + ocol) * 4, 0, 16);
        }
    }
    asm volatile(".Lcode_mark1:");
}

extern "C" void kernel_launch(void* const* d_in, const int* in_sizes, int n_in, void* d_out, int out_size, void* d_ws,
                              size_t ws_size, hipStream_t stream) {
    (void)in_sizes; (void)n_in; (void)out_size; (void)ws_size;
    (void)hipFuncSetAttribute((const void*)s5_mlp_fused, hipFuncAttributeMaxDynamicSharedMemorySize, LDS_BYTES);
    s5_mlp_fused<<<256, 512, LDS_BYTES, stream>>>(
        (const u16*)d_in[0], (const u16*)d_in[1], (const u16*)d_in[2], (const u16*)d_in[3], (const u16*)d_in[4],
        (const u16*)d_in[5], (const u16*)d_in[6], (const u16*)d_in[7], (const u16*)d_in[8], (const u16*)d_in[10],
        (const u16*)d_in[11], (const u16*)d_in[12], (const u16*)d_in[13], (float*)d_out, (char*)d_ws);
}
```
